```python
import torch

def _softmax(x, axis=-1):
    return torch.softmax(x, dim=axis)
def _gelu(x):
    return torch.nn.functional.gelu(x, approximate="tanh")
def _silu(x):
    return torch.nn.functional.silu(x)
def _sigmoid(x):
    return torch.sigmoid(x)
def _layernorm(x, axis=-1, eps=1e-5):
    m = x.mean(dim=axis, keepdim=True)
    v = x.var(dim=axis, keepdim=True, unbiased=False)
    return (x - m) / torch.sqrt(v + eps)
def _scatter_add(tgt, idx, src, axis=0):
    return tgt.clone().index_add_(axis, idx, src)
def _segment_sum(src, seg, n):
    out = torch.zeros((n,) + src.shape[1:], dtype=src.dtype)
    return out.index_add_(0, seg, src)
def _pad(x, pw):
    flat = tuple(v for pair in reversed(pw) for v in pair)
    return torch.nn.functional.pad(x, flat)

kernel_name = 'gated_mix__sliding_block_cached__geglu_ffn__decode'

def setup_inputs():

    _w = (lambda a: (lambda t: t.to(torch.bfloat16) if t.is_floating_point() else t)(torch.as_tensor(a)))
    def _A_setup():
        B, Sq, D = 2, 1, 1024
        Hq, Hkv, Sk, Dh = 32, 4, 32768, 128
        assert Hq % Hkv == 0, (Hq, Hkv)
        return {
            "x": _w((lambda *s: torch.randn(*s, dtype=torch.bfloat16) * 0.05)(B, Sq, D)),
            "k_cache": _w((lambda *s: torch.randn(*s, dtype=torch.bfloat16) * 6.4)(B, Hkv, Sk, Dh)),
            "v_cache": _w((lambda *s: torch.randn(*s, dtype=torch.bfloat16) * 0.4)(B, Hkv, Sk, Dh)),
            "wq": _w((lambda *s: torch.randn(*s, dtype=torch.bfloat16) * 6.4)(D, Hq * Dh) * (D ** -0.5)),
            "wo": _w((lambda *s: torch.randn(*s, dtype=torch.bfloat16) * 0.4)(Hq * Dh, D) * ((Hq * Dh) ** -0.5)),
        }
    _A_in = _A_setup()

    def _B_setup():
        B, S, D, F = 2, 1, 1024, 3584
        return {
            "x": _w((lambda *s: torch.randn(*s, dtype=torch.bfloat16) * 0.05)(B, S, D)),
            "w_gate": _w((lambda *s: torch.randn(*s, dtype=torch.bfloat16) * 0.05)(D, F)),
            "w_up": _w((lambda *s: torch.randn(*s, dtype=torch.bfloat16) * 0.05)(D, F)),
            "w_down": _w((lambda *s: torch.randn(*s, dtype=torch.bfloat16) * 0.05)(F, D)),
        }
    _B_in = _B_setup()

    return {
        "A_x": _A_in["x"],
        "A_k_cache": _A_in["k_cache"],
        "A_v_cache": _A_in["v_cache"],
        "A_wq": _A_in["wq"],
        "A_wo": _A_in["wo"],
        "B_x": _B_in["x"],
        "B_w_gate": _B_in["w_gate"],
        "B_w_up": _B_in["w_up"],
        "B_w_down": _B_in["w_down"],
        "gate_w": _w((lambda *s: torch.randn(*s, dtype=torch.bfloat16) * 0.05)(1024) * 16.0),
    }

def reference(A_x, A_k_cache, A_v_cache, A_wq, A_wo, B_x, B_w_gate, B_w_up, B_w_down, gate_w):
    def _A_ref(x, k_cache, v_cache, wq, wo):
        B, Sq, D = x.shape
        Hq, Dh = 32, 128
        Hkv, Sk = k_cache.shape[1], k_cache.shape[2]
        W = min(4096, max(256, Sk // 4))
        rep = Hq // Hkv
        q = (lambda x, *d: x.permute(*d))((x @ wq).reshape(B, Sq, Hq, Dh), 0, 2, 1, 3)
        k = (lambda x, n, axis: torch.repeat_interleave(x, n, dim=axis))(k_cache[:, :, Sk - W:, :], rep, axis=1)
        v = (lambda x, n, axis: torch.repeat_interleave(x, n, dim=axis))(v_cache[:, :, Sk - W:, :], rep, axis=1)
        scores = torch.einsum("bhqd,bhkd->bhqk", q, k) * (Dh ** -0.5)
        p = _softmax(scores, axis=-1)
        o = torch.einsum("bhqk,bhkd->bhqd", p, v)
        return (lambda x, *d: x.permute(*d))(o, 0, 2, 1, 3).reshape(B, Sq, Hq * Dh) @ wo

    def _B_ref(x, w_gate, w_up, w_down):
        return (_gelu(x @ w_gate) * (x @ w_up)) @ w_down

    x = A_x
    g = _sigmoid((x * gate_w).sum(-1, keepdims=True))
    a = _A_ref(x, A_k_cache, A_v_cache, A_wq, A_wo)
    b = _B_ref(x, B_w_gate, B_w_up, B_w_down)
    return g * a + (1 - g) * b
```

```cpp
#include <hip/hip_runtime.h>
#include <hip/hip_bf16.h>

typedef unsigned u32;
typedef unsigned u32x4 __attribute__((ext_vector_type(4)));
typedef unsigned u32x2 __attribute__((ext_vector_type(2)));
typedef float f32x4 __attribute__((ext_vector_type(4)));
typedef short bf16x8 __attribute__((ext_vector_type(8)));
typedef unsigned short u16;
#define DEVI __device__ __forceinline__

constexpr int DM = 1024, NQ = 4096, SK = 32768, WW = 4096, DH = 128, FF = 3584;
constexpr int QP_OFF = 4096;
constexpr int FP_OFF = QP_OFF + 2 * 4 * 4096 * 4;
constexpr int OP_OFF = FP_OFF + 64 * 4 * 256 * 4;
constexpr int ML_OFF = OP_OFF + 256 * 1024 * 4;
constexpr int RP_OFF = ML_OFF + 8 * 8 * 32 * 16;
constexpr int WS_BYTES = RP_OFF + 4 * 64 * 512 * 4;
constexpr int AUX_SC1 = 16;
constexpr u32 SPIN_CAP = 1u << 20;

#define ST(i) do {} while (0)
#define PIN() __builtin_amdgcn_sched_barrier(0)
#define BAR(e) u32 e = __builtin_amdgcn_readfirstlane(ev); asm volatile("s_waitcnt lgkmcnt(0)\n\ts_barrier" : "+s"(e) :: "memory")
#define VMW(n) do { asm volatile("s_waitcnt vmcnt(%0)" :: "i"(n) : "memory"); PIN(); } while (0)

DEVI u32x4 ldnt(const u16* p) { return __builtin_nontemporal_load((const u32x4*)p); }
DEVI float asf(u32 u) { return __builtin_bit_cast(float, u); }
DEVI u32 asu(float f) { return __builtin_bit_cast(u32, f); }
DEVI u32 tagf(float f, u32 t) { return ((asu(f) + 0x80u) & 0xFFFFFF00u) | t; }
DEVI float untag(u32 u) { return asf(u & 0xFFFFFF00u); }
DEVI u32 bad4(u32x4 v, u32 t) { return (v.x ^ t) | (v.y ^ t) | (v.z ^ t) | (v.w ^ t); }
DEVI u32 pkbf(float lo, float hi) { u32 r; asm("v_cvt_pk_bf16_f32 %0, %1, %2" : "=v"(r) : "v"(lo), "v"(hi)); return r; }
DEVI f32x4 mfma(u32x4 a, u32x4 b, f32x4 c) {
  return __builtin_amdgcn_mfma_f32_16x16x32_bf16(__builtin_bit_cast(bf16x8, a), __builtin_bit_cast(bf16x8, b), c, 0, 0, 0);
}
template <int N> DEVI float ror(float x) { return asf(__builtin_amdgcn_update_dpp(0, asu(x), 0x120 + N, 0xf, 0xf, false)); }
DEVI float sum32(float a, float b) { auto r = __builtin_amdgcn_permlane32_swap(asu(a), asu(b), false, false); return asf(r[0]) + asf(r[1]); }
DEVI float sum16(float a, float b) { auto r = __builtin_amdgcn_permlane16_swap(asu(a), asu(b), false, false); return asf(r[0]) + asf(r[1]); }
DEVI float max32(float a) { auto r = __builtin_amdgcn_permlane32_swap(asu(a), asu(a), false, false); return fmaxf(asf(r[0]), asf(r[1])); }
DEVI float max16(float a) { auto r = __builtin_amdgcn_permlane16_swap(asu(a), asu(a), false, false); return fmaxf(asf(r[0]), asf(r[1])); }

DEVI u32x4 colfrag(const u32x4 (&r)[8], int c) {
  const u32 sel = (c & 1) ? 0x07060302u : 0x05040100u;
  const int d = c >> 1;
  u32x4 f;
  f.x = __builtin_amdgcn_perm(r[1][d], r[0][d], sel);
  f.y = __builtin_amdgcn_perm(r[3][d], r[2][d], sel);
  f.z = __builtin_amdgcn_perm(r[5][d], r[4][d], sel);
  f.w = __builtin_amdgcn_perm(r[7][d], r[6][d], sel);
  return f;
}
DEVI void gemv_tile(const u32x4 (&r)[8], u32x4 a, int lane, float (&res)[8]) {
  const bool lo = (lane & 8) == 0;
#pragma unroll
  for (int c = 0; c < 8; ++c) {
    f32x4 d = mfma(a, colfrag(r, c), (f32x4){0.f, 0.f, 0.f, 0.f});
    res[c] = (lo ? d[0] : d[3]) + ror<8>(lo ? d[1] : d[2]);
  }
}
DEVI void lds_signal(u32* cnt, int lane) {
  asm volatile("s_waitcnt lgkmcnt(0)" ::: "memory");
  if (lane == 0) __hip_atomic_fetch_add(cnt, 1u, __ATOMIC_RELAXED, __HIP_MEMORY_SCOPE_WORKGROUP);
  asm volatile("" ::: "memory");
}
DEVI void lds_wait(u32* cnt, u32 target) {
  for (u32 s = 0; s < (SPIN_CAP << 6); ++s) {
    asm volatile("" ::: "memory");
    u32 v = __hip_atomic_load(cnt, __ATOMIC_RELAXED, __HIP_MEMORY_SCOPE_WORKGROUP);
    if (__builtin_amdgcn_readfirstlane(v) >= target) break;
  }
  asm volatile("" ::: "memory");
}

struct __attribute__((aligned(16))) Smem {
  float opart[4][8][128];
  float fpart[4][256];
  float qpart[4][128];
  float apart[4][128];
  float o2[4][64];
  float ml[4][8][2];
  u32 qbf[512];
  u32 hbf[64];
  float gate[2];
  u32 cnt[6];
};
enum { C_Q = 0, C_O = 1, C_O2 = 2, C_A = 3, C_F = 4, C_H = 5 };

__global__ void __launch_bounds__(576, 3)
fused_decode(const u16* x, const u16* kc, const u16* vc, const u16* wq, const u16* wo, const u16* wg, const u16* wu,
             const u16* wd, const u16* gw, u16* out, u32* ws) {
  __shared__ Smem sm;
  const int lane = threadIdx.x & 63;
  const int wave = __builtin_amdgcn_readfirstlane(threadIdx.x >> 6);
  const int c = blockIdx.x, j = c & 63, cg = c >> 6;
  const int w = wave & 3;
  const bool heavy = j < 56;
  ST(0);
  const __amdgpu_buffer_rsrc_t rs = __builtin_amdgcn_make_buffer_rsrc((void*)ws, 0, WS_BYTES, 0x00020000);
  const int m16 = lane & 15, g4 = lane >> 4;
  const int arow = (g4 * 2 + ((m16 >> 1) & 1)) * 8;
  const int trow = (lane >> 3) * 8, tcol = (lane & 7) * 8;
  const u32 ev = __hip_atomic_load(ws, __ATOMIC_RELAXED, __HIP_MEMORY_SCOPE_AGENT);
  PIN();

  if (wave < 4) {
    __builtin_amdgcn_s_setprio(2);
    const int bk = c & 7, sp = c >> 3;
    u32x4 xa = *(const u32x4*)(x + (m16 & 1) * DM + cg * 256 + w * 64 + arow);
    u32x4 rq[8], rk[8], rv[8], ro[8];
    {
      const u16* p = wq + (size_t)(cg * 256 + w * 64 + trow) * NQ + j * 64 + tcol;
#pragma unroll
      for (int i = 0; i < 8; ++i) rq[i] = ldnt(p + i * NQ);
    }
    PIN();
    const size_t kvb = ((size_t)bk * SK + (SK - WW) + sp * 128 + w * 32) * DH;
    {
      const u16* p = kc + kvb + (8 * (m16 >> 2) + (m16 & 3)) * DH + g4 * 8;
#pragma unroll
      for (int i = 0; i < 8; ++i) rk[i] = ldnt(p + (i >> 2) * 4 * DH + (i & 3) * 32);
    }
    PIN();
    VMW(8);
    ST(1);
    {
      const u16* p = vc + kvb + g4 * 8 * DH + m16 * 8;
#pragma unroll
      for (int i = 0; i < 8; ++i) rv[i] = ldnt(p + i * DH);
    }
    PIN();
    {
      float res[8];
      gemv_tile(rq, xa, lane, res);
      if (lane < 16) {
        *(f32x4*)&sm.qpart[w][lane * 8] = (f32x4){res[0], res[1], res[2], res[3]};
        *(f32x4*)&sm.qpart[w][lane * 8 + 4] = (f32x4){res[4], res[5], res[6], res[7]};
      }
    }
    BAR(epoch);
    const u32 tag = (epoch + 1u) & 0xFFu;
    if (lane < 8) {
      const int e = w * 32 + lane * 4;
      f32x4 s = *(f32x4*)&sm.qpart[0][e] + *(f32x4*)&sm.qpart[1][e] + *(f32x4*)&sm.qpart[2][e] + *(f32x4*)&sm.qpart[3][e];
      u32x4 o = {tagf(s[0], tag), tagf(s[1], tag), tagf(s[2], tag), tagf(s[3], tag)};
      __builtin_amdgcn_raw_buffer_store_b128(o, rs, QP_OFF + (((e >> 6) * 4 + cg) * NQ + j * 64 + (e & 63)) * 4, 0, AUX_SC1);
    }
    ST(2);
    {
      const int off = QP_OFF + ((bk >> 2) * 4 * NQ + (bk & 3) * 1024 + w * 256 + lane * 4) * 4;
      u32x4 v[4];
      for (u32 s = 0; s < SPIN_CAP; ++s) {
        asm volatile("" ::: "memory");
#pragma unroll
        for (int i = 0; i < 4; ++i) v[i] = __builtin_amdgcn_raw_buffer_load_b128(rs, off + i * NQ * 4, 0, AUX_SC1);
        const u32 b = bad4(v[0], tag) | bad4(v[1], tag) | bad4(v[2], tag) | bad4(v[3], tag);
        if (__all((b & 0xFFu) == 0)) break;
      }
      float q[4];
#pragma unroll
      for (int e = 0; e < 4; ++e) q[e] = (untag(v[0][e]) + untag(v[1][e])) + (untag(v[2][e]) + untag(v[3][e]));
      *(u32x2*)&sm.qbf[w * 128 + lane * 2] = (u32x2){pkbf(q[0], q[1]), pkbf(q[2], q[3])};
    }
    lds_signal(&sm.cnt[C_Q], lane);
    PIN();
    ST(3);
    {
      const u16* p = wo + (size_t)(j * 64 + trow) * DM + cg * 256 + w * 64 + tcol;
#pragma unroll
      for (int i = 0; i < 8; ++i) ro[i] = ldnt(p + i * DM);
    }
    PIN();
    lds_wait(&sm.cnt[C_Q], 4);
    {
      f32x4 s0 = {0.f, 0.f, 0.f, 0.f}, s1 = {0.f, 0.f, 0.f, 0.f};
#pragma unroll
      for (int t = 0; t < 4; ++t) {
        const u32x4 qf = *(const u32x4*)&sm.qbf[(m16 & 7) * 64 + t * 16 + g4 * 4];
        s0 = mfma(rk[t], qf, s0);
        s1 = mfma(rk[4 + t], qf, s1);
      }
      const float sc = 0.08838834764831845f * 1.4426950408889634f;
      float mx = fmaxf(fmaxf(fmaxf(s0[0], s0[1]), fmaxf(s0[2], s0[3])), fmaxf(fmaxf(s1[0], s1[1]), fmaxf(s1[2], s1[3])));
      mx = max32(max16(mx)) * sc;
      float p[8];
#pragma unroll
      for (int e = 0; e < 4; ++e) {
        p[e] = __builtin_amdgcn_exp2f(fmaf(s0[e], sc, -mx));
        p[4 + e] = __builtin_amdgcn_exp2f(fmaf(s1[e], sc, -mx));
      }
      float l = ((p[0] + p[1]) + (p[2] + p[3])) + ((p[4] + p[5]) + (p[6] + p[7]));
      l = sum16(l, l);
      l = sum32(l, l);
      const u32x4 pf = {pkbf(p[0], p[1]), pkbf(p[2], p[3]), pkbf(p[4], p[5]), pkbf(p[6], p[7])};
      f32x4 d[8];
#pragma unroll
      for (int cc = 0; cc < 8; ++cc) d[cc] = mfma(pf, colfrag(rv, cc), (f32x4){0.f, 0.f, 0.f, 0.f});
      const bool lo = lane < 32;
#pragma unroll
      for (int r = 0; r < 4; ++r)
        *(f32x4*)&sm.opart[w][(g4 & 1) * 4 + r][m16 * 8 + (lo ? 0 : 4)] =
            (f32x4){lo ? d[0][r] : d[4][r], lo ? d[1][r] : d[5][r], lo ? d[2][r] : d[6][r], lo ? d[3][r] : d[7][r]};
      if (lane < 8) { sm.ml[w][lane][0] = mx; sm.ml[w][lane][1] = l; }
    }
    lds_signal(&sm.cnt[C_O], lane);
    lds_wait(&sm.cnt[C_O], 4);
    {
      const int h = 2 * w + (lane >> 5), dd = (lane & 31) * 4;
      float mw[4], lw[4];
      f32x4 ow[4];
#pragma unroll
      for (int i = 0; i < 4; ++i) { mw[i] = sm.ml[i][h][0]; lw[i] = sm.ml[i][h][1]; ow[i] = *(f32x4*)&sm.opart[i][h][dd]; }
      const u32 mt = tagf(fmaxf(fmaxf(mw[0], mw[1]), fmaxf(mw[2], mw[3])), tag);
      const float M = untag(mt);
      f32x4 o = {0.f, 0.f, 0.f, 0.f};
      float L = 0.f;
#pragma unroll
      for (int i = 0; i < 4; ++i) { const float f = __builtin_amdgcn_exp2f(mw[i] - M); o += ow[i] * f; L += lw[i] * f; }
      u32x4 ot = {tagf(o[0], tag), tagf(o[1], tag), tagf(o[2], tag), tagf(o[3], tag)};
      __builtin_amdgcn_raw_buffer_store_b128(ot, rs, OP_OFF + (c * 1024 + h * 128 + dd) * 4, 0, AUX_SC1);
      if ((lane & 31) == 0)
        __builtin_amdgcn_raw_buffer_store_b128((u32x4){mt, tagf(L, tag), epoch + 1u, epoch + 1u}, rs, ML_OFF + ((bk * 8 + h) * 32 + sp) * 16, 0, AUX_SC1);
    }
    ST(4);
    {
      const int pbk = (w >> 1) * 4 + (j >> 4), hh = (j >> 1) & 7;
      const int s0 = (w & 1) * 16 + g4;
      const int off = OP_OFF + ((pbk + 8 * s0) * 1024 + hh * 128 + (j & 1) * 64 + m16 * 4) * 4;
      const int moff = ML_OFF + ((pbk * 8 + hh) * 32 + (lane & 31)) * 16;
      u32x4 v[4];
      u32x4 ml;
      for (u32 s = 0; s < SPIN_CAP; ++s) {
        asm volatile("" ::: "memory");
        ml = __builtin_amdgcn_raw_buffer_load_b128(rs, moff, 0, AUX_SC1);
#pragma unroll
        for (int i = 0; i < 4; ++i) v[i] = __builtin_amdgcn_raw_buffer_load_b128(rs, off + i * 32 * 4096, 0, AUX_SC1);
        const u32 b = bad4(v[0], tag) | bad4(v[1], tag) | bad4(v[2], tag) | bad4(v[3], tag) | (ml.x ^ tag) | (ml.y ^ tag);
        if (__all(((b & 0xFFu) | (ml.z ^ (epoch + 1u)) | (ml.w ^ (epoch + 1u))) == 0)) break;
      }
      ST(5);
      const float ms = untag(ml.x);
      float M = fmaxf(ms, ror<8>(ms));
      M = fmaxf(M, ror<4>(M)); M = fmaxf(M, ror<2>(M)); M = fmaxf(M, ror<1>(M));
      M = max16(M);
      const float f = __builtin_amdgcn_exp2f(ms - M);
      float L = f * untag(ml.y);
      L += ror<8>(L); L += ror<4>(L); L += ror<2>(L); L += ror<1>(L);
      L = sum16(L, L);
      const float wt = f / L;
      float o[4] = {0.f, 0.f, 0.f, 0.f};
#pragma unroll
      for (int i = 0; i < 4; ++i) {
        const float wi = __shfl(wt, s0 + i * 4, 64);
#pragma unroll
        for (int e = 0; e < 4; ++e) o[e] = fmaf(wi, untag(v[i][e]), o[e]);
      }
      const float o01 = sum32(o[0], o[2]), o23 = sum32(o[1], o[3]);
      const float oo = sum16(o01, o23);
      sm.o2[w][m16 * 4 + g4] = oo;
    }
    lds_signal(&sm.cnt[C_O2], lane);
    lds_wait(&sm.cnt[C_O2], 4);
    {
      const int b2 = (m16 & 1) * 2;
      const f32x4 a0 = *(f32x4*)&sm.o2[b2][arow] + *(f32x4*)&sm.o2[b2 + 1][arow];
      const f32x4 a1 = *(f32x4*)&sm.o2[b2][arow + 4] + *(f32x4*)&sm.o2[b2 + 1][arow + 4];
      const u32x4 oa = {pkbf(a0[0], a0[1]), pkbf(a0[2], a0[3]), pkbf(a1[0], a1[1]), pkbf(a1[2], a1[3])};
      float res[8];
      gemv_tile(ro, oa, lane, res);
      const float gg = sm.gate[(lane >> 3) & 1];
      if (lane < 16) {
        if (heavy) {
          *(f32x4*)&sm.apart[w][lane * 8] = (f32x4){res[0], res[1], res[2], res[3]} * gg;
          *(f32x4*)&sm.apart[w][lane * 8 + 4] = (f32x4){res[4], res[5], res[6], res[7]} * gg;
        } else {
          const int off = RP_OFF + ((c * 2 + (lane >> 3)) * 256 + w * 64 + tcol) * 4;
          __builtin_amdgcn_raw_buffer_store_b128((u32x4){tagf(res[0] * gg, tag), tagf(res[1] * gg, tag), tagf(res[2] * gg, tag), tagf(res[3] * gg, tag)}, rs, off, 0, AUX_SC1);
          __builtin_amdgcn_raw_buffer_store_b128((u32x4){tagf(res[4] * gg, tag), tagf(res[5] * gg, tag), tagf(res[6] * gg, tag), tagf(res[7] * gg, tag)}, rs, off + 16, 0, AUX_SC1);
        }
      }
    }
    lds_signal(&sm.cnt[C_A], lane);
    ST(6);
  } else if (wave < 8) {
    if (heavy) {
      u32x4 xa = *(const u32x4*)(x + (m16 & 1) * DM + cg * 256 + w * 64 + arow);
      u32x4 rg[8], ru[8], rd[8];
      const size_t fo = (size_t)(cg * 256 + w * 64 + trow) * FF + j * 64 + tcol;
      const u16* pd = wd + (size_t)(j * 64 + trow) * DM + cg * 256 + w * 64 + tcol;
      BAR(epoch);
      const u32 tag = (epoch + 1u) & 0xFFu;
      ST(1);
#pragma unroll
      for (int i = 0; i < 8; ++i) rg[i] = ldnt(wg + fo + i * FF);
      PIN();
#pragma unroll
      for (int i = 0; i < 8; ++i) { VMW(7); ru[i] = ldnt(wu + fo + i * FF); PIN(); }
      ST(2);
      float res[8];
      gemv_tile(rg, xa, lane, res);
      if (lane < 16) {
        *(f32x4*)&sm.fpart[w][lane * 8] = (f32x4){res[0], res[1], res[2], res[3]};
        *(f32x4*)&sm.fpart[w][lane * 8 + 4] = (f32x4){res[4], res[5], res[6], res[7]};
      }
      PIN();
#pragma unroll
      for (int i = 0; i < 8; ++i) { VMW(7); rd[i] = ldnt(pd + i * DM); PIN(); }
      ST(3);
      gemv_tile(ru, xa, lane, res);
      if (lane < 16) {
        *(f32x4*)&sm.fpart[w][128 + lane * 8] = (f32x4){res[0], res[1], res[2], res[3]};
        *(f32x4*)&sm.fpart[w][128 + lane * 8 + 4] = (f32x4){res[4], res[5], res[6], res[7]};
      }
      lds_signal(&sm.cnt[C_F], lane);
      u32x4 fr[8];
#pragma unroll
      for (int cc = 0; cc < 8; ++cc) { fr[cc] = colfrag(rd, cc); asm volatile("" : "+v"(fr[cc])); }
      ST(4);
      lds_wait(&sm.cnt[C_H], 1);
      ST(5);
      const u32x4 ha = *(const u32x4*)&sm.hbf[(m16 & 1) * 32 + (arow >> 1)];
      const bool lo = (lane & 8) == 0;
#pragma unroll
      for (int cc = 0; cc < 8; ++cc) {
        f32x4 d = mfma(ha, fr[cc], (f32x4){0.f, 0.f, 0.f, 0.f});
        res[cc] = (lo ? d[0] : d[3]) + ror<8>(lo ? d[1] : d[2]);
      }
      const float g1 = 1.f - sm.gate[(lane >> 3) & 1];
      lds_wait(&sm.cnt[C_A], 4);
      ST(6);
      if (lane < 16) {
        const f32x4 a0 = *(f32x4*)&sm.apart[w][lane * 8], a1 = *(f32x4*)&sm.apart[w][lane * 8 + 4];
        const int off = RP_OFF + ((c * 2 + (lane >> 3)) * 256 + w * 64 + tcol) * 4;
        __builtin_amdgcn_raw_buffer_store_b128((u32x4){tagf(fmaf(res[0], g1, a0[0]), tag), tagf(fmaf(res[1], g1, a0[1]), tag), tagf(fmaf(res[2], g1, a0[2]), tag), tagf(fmaf(res[3], g1, a0[3]), tag)}, rs, off, 0, AUX_SC1);
        __builtin_amdgcn_raw_buffer_store_b128((u32x4){tagf(fmaf(res[4], g1, a1[0]), tag), tagf(fmaf(res[5], g1, a1[1]), tag), tagf(fmaf(res[6], g1, a1[2]), tag), tagf(fmaf(res[7], g1, a1[3]), tag)}, rs, off + 16, 0, AUX_SC1);
      }
      ST(7);
    } else {
      BAR(epoch);
      const u32 tag = (epoch + 1u) & 0xFFu;
      const int b = w & 1, col = (j - 56) * 32 + (w >> 1) * 16;
      const int off = RP_OFF + (((cg * 64 + (lane >> 2)) * 2 + b) * 256 + col + (lane & 3) * 4) * 4;
      lds_wait(&sm.cnt[C_A], 4);
      ST(1);
      u32x4 v[4];
      for (u32 s = 0; s < SPIN_CAP; ++s) {
        asm volatile("" ::: "memory");
#pragma unroll
        for (int i = 0; i < 4; ++i) v[i] = __builtin_amdgcn_raw_buffer_load_b128(rs, off + i * 16 * 2048, 0, AUX_SC1);
        const u32 bb = bad4(v[0], tag) | bad4(v[1], tag) | bad4(v[2], tag) | bad4(v[3], tag);
        if (__all((bb & 0xFFu) == 0)) break;
      }
      ST(8);
      float o[4];
#pragma unroll
      for (int e = 0; e < 4; ++e) o[e] = (untag(v[0][e]) + untag(v[1][e])) + (untag(v[2][e]) + untag(v[3][e]));
      float t = sum16(sum32(o[0], o[2]), sum32(o[1], o[3]));
      t += ror<8>(t);
      t += ror<4>(t);
      if (m16 < 4) out[b * DM + cg * 256 + col + m16 * 4 + g4] = (u16)pkbf(t, t);
      if (c == 63 && w == 0 && lane == 0) __hip_atomic_store(ws, epoch + 1u, __ATOMIC_RELAXED, __HIP_MEMORY_SCOPE_AGENT);
      ST(9);
    }
  } else {
    __builtin_amdgcn_s_setprio(3);
    if (lane < 6) sm.cnt[lane] = 0u;
    {
      float acc[2] = {0.f, 0.f};
#pragma unroll
      for (int i = 0; i < 2; ++i) {
        const u32x4 gv = *(const u32x4*)(gw + i * 512 + lane * 8);
#pragma unroll
        for (int b = 0; b < 2; ++b) {
          const u32x4 xv = *(const u32x4*)(x + b * DM + i * 512 + lane * 8);
#pragma unroll
          for (int e = 0; e < 4; ++e) {
            acc[b] = fmaf(asf(gv[e] << 16), asf(xv[e] << 16), acc[b]);
            acc[b] = fmaf(asf(gv[e] & 0xFFFF0000u), asf(xv[e] & 0xFFFF0000u), acc[b]);
          }
        }
      }
      float t = sum32(acc[0], acc[1]);
      t = sum16(t, t);
      t += ror<8>(t); t += ror<4>(t); t += ror<2>(t); t += ror<1>(t);
      if ((lane & 31) == 0) sm.gate[lane >> 5] = 1.f / (1.f + __expf(-t));
    }
    BAR(epoch);
    const u32 tag = (epoch + 1u) & 0xFFu;
    if (heavy) {
      lds_wait(&sm.cnt[C_F], 4);
      ST(1);
      const f32x4 s = (*(f32x4*)&sm.fpart[0][lane * 4] + *(f32x4*)&sm.fpart[1][lane * 4]) + (*(f32x4*)&sm.fpart[2][lane * 4] + *(f32x4*)&sm.fpart[3][lane * 4]);
      const int off = FP_OFF + (j * 1024 + lane * 4) * 4;
      __builtin_amdgcn_raw_buffer_store_b128((u32x4){tagf(s[0], tag), tagf(s[1], tag), tagf(s[2], tag), tagf(s[3], tag)}, rs, off + cg * 1024, 0, AUX_SC1);
      u32x4 v[4];
      for (u32 sp = 0; sp < SPIN_CAP; ++sp) {
        asm volatile("" ::: "memory");
#pragma unroll
        for (int i = 0; i < 4; ++i) v[i] = __builtin_amdgcn_raw_buffer_load_b128(rs, off + i * 1024, 0, AUX_SC1);
        const u32 bb = bad4(v[0], tag) | bad4(v[1], tag) | bad4(v[2], tag) | bad4(v[3], tag);
        if (__all((bb & 0xFFu) == 0)) break;
      }
      ST(2);
      float h[4];
#pragma unroll
      for (int e = 0; e < 4; ++e) {
        const float t = (untag(v[0][e]) + untag(v[1][e])) + (untag(v[2][e]) + untag(v[3][e]));
        auto r = __builtin_amdgcn_permlane32_swap(asu(t), asu(t), false, false);
        const float gt = asf(r[0]), up = asf(r[1]);
        const float y = 0.7978845608028654f * (gt + 0.044715f * gt * gt * gt);
        const float th = 1.f - 2.f / (__expf(2.f * y) + 1.f);
        h[e] = 0.5f * gt * (1.f + th) * up;
      }
      if (lane < 32) *(u32x2*)&sm.hbf[lane * 2] = (u32x2){pkbf(h[0], h[1]), pkbf(h[2], h[3])};
      lds_signal(&sm.cnt[C_H], lane);
      ST(3);
    }
  }
}

extern "C" void kernel_launch(void* const* d_in, const int* in_sizes, int n_in, void* d_out, int out_size, void* d_ws,
                              size_t ws_size, hipStream_t stream) {
  fused_decode<<<256, 576, 0, stream>>>((const u16*)d_in[0], (const u16*)d_in[1], (const u16*)d_in[2], (const u16*)d_in[3],
                                        (const u16*)d_in[4], (const u16*)d_in[6], (const u16*)d_in[7], (const u16*)d_in[8],
                                        (const u16*)d_in[9], (u16*)d_out, (u32*)d_ws);
}
```

```cpp
#include <hip/hip_runtime.h>
#include <hip/hip_bf16.h>

typedef unsigned u32;
typedef unsigned u32x4 __attribute__((ext_vector_type(4)));
typedef unsigned u32x2 __attribute__((ext_vector_type(2)));
typedef float f32x4 __attribute__((ext_vector_type(4)));
typedef short bf16x8 __attribute__((ext_vector_type(8)));
typedef unsigned short u16;
#define DEVI __device__ __forceinline__

constexpr int DM = 1024, NQ = 4096, SK = 32768, WW = 4096, DH = 128, FF = 3584;
constexpr int QP_OFF = 4096;
constexpr int FP_OFF = QP_OFF + 2 * 4 * 4096 * 4;
constexpr int OP_OFF = FP_OFF + 64 * 4 * 256 * 4;
constexpr int ML_OFF = OP_OFF + 256 * 1024 * 4;
constexpr int RP_OFF = ML_OFF + 8 * 8 * 32 * 16;
constexpr int WS_BYTES = RP_OFF + 4 * 64 * 512 * 4;
constexpr int AUX_SC1 = 16;
constexpr u32 SPIN_CAP = 1u << 20;

#define GA
#define GP(i)
#define ST(i) do {} while (0)
#define PIN() __builtin_amdgcn_sched_barrier(0)
#define BAR() asm volatile("s_waitcnt lgkmcnt(0)\n\ts_barrier" ::: "memory")
#define AWIN 6
#define FWIN 6
#define TOK1 4
#define TOK2 1
#define D1 16
#define D2 16
#define D4 0
#define D3 0
#define FA 1
#define HA1 1
#define HA2 1
#define VMW(n) do { asm volatile("s_waitcnt vmcnt(%0)" :: "i"(n) : "memory"); PIN(); } while (0)

DEVI u32x4 ldnt(const u16* p) { return __builtin_nontemporal_load((const u32x4*)p); }
DEVI float asf(u32 u) { return __builtin_bit_cast(float, u); }
DEVI u32 asu(float f) { return __builtin_bit_cast(u32, f); }
DEVI u32 tagf(float f, u32 t) { return ((asu(f) + 0x80u) & 0xFFFFFF00u) | t; }
DEVI float untag(u32 u) { return asf(u & 0xFFFFFF00u); }
DEVI u32 bad4(u32x4 v, u32 t) { return (v.x ^ t) | (v.y ^ t) | (v.z ^ t) | (v.w ^ t); }
typedef float f32x2 __attribute__((ext_vector_type(2)));
typedef __bf16 bf16x2 __attribute__((ext_vector_type(2)));
DEVI u32 pkbf(float lo, float hi) { return __builtin_bit_cast(u32, __builtin_convertvector((f32x2){lo, hi}, bf16x2)); }
DEVI void rb2(float& a, float& b) { const u32 r = pkbf(a, b); a = asf(r << 16); b = asf(r & 0xFFFF0000u); }
DEVI float rb1(float a) { return asf(pkbf(a, a) << 16); }
DEVI f32x4 mfma(u32x4 a, u32x4 b, f32x4 c) {
  return __builtin_amdgcn_mfma_f32_16x16x32_bf16(__builtin_bit_cast(bf16x8, a), __builtin_bit_cast(bf16x8, b), c, 0, 0, 0);
}
template <int N> DEVI float ror(float x) { return asf(__builtin_amdgcn_update_dpp(0, asu(x), 0x120 + N, 0xf, 0xf, false)); }
template <int N> DEVI float bcast(float x) { return asf(__builtin_amdgcn_update_dpp(0, asu(x), 0x150 + N, 0xf, 0xf, false)); }
DEVI float sum32(float a, float b) { auto r = __builtin_amdgcn_permlane32_swap(asu(a), asu(b), false, false); return asf(r[0]) + asf(r[1]); }
DEVI float sum16(float a, float b) { auto r = __builtin_amdgcn_permlane16_swap(asu(a), asu(b), false, false); return asf(r[0]) + asf(r[1]); }
DEVI float max32(float a) { auto r = __builtin_amdgcn_permlane32_swap(asu(a), asu(a), false, false); return fmaxf(asf(r[0]), asf(r[1])); }
DEVI float max16(float a) { auto r = __builtin_amdgcn_permlane16_swap(asu(a), asu(a), false, false); return fmaxf(asf(r[0]), asf(r[1])); }

DEVI u32x4 colfrag(const u32x4 (&r)[8], int c) {
  const u32 sel = (c & 1) ? 0x07060302u : 0x05040100u;
  const int d = c >> 1;
  u32x4 f;
  f.x = __builtin_amdgcn_perm(r[1][d], r[0][d], sel);
  f.y = __builtin_amdgcn_perm(r[3][d], r[2][d], sel);
  f.z = __builtin_amdgcn_perm(r[5][d], r[4][d], sel);
  f.w = __builtin_amdgcn_perm(r[7][d], r[6][d], sel);
  return f;
}
DEVI void gemv_tile(const u32x4 (&r)[8], u32x4 a, int lane, float (&res)[8]) {
  const bool lo = (lane & 8) == 0;
#pragma unroll
  for (int c = 0; c < 8; ++c) {
    f32x4 d = mfma(a, colfrag(r, c), (f32x4){0.f, 0.f, 0.f, 0.f});
    res[c] = (lo ? d[0] : d[3]) + ror<8>(lo ? d[1] : d[2]);
  }
}
DEVI void lds_signal(u32* cnt, int lane) {
  asm volatile("s_waitcnt lgkmcnt(0)" ::: "memory");
  if (lane == 0) __hip_atomic_fetch_add(cnt, 1u, __ATOMIC_RELAXED, __HIP_MEMORY_SCOPE_WORKGROUP);
  asm volatile("" ::: "memory");
}
DEVI void lds_wait(u32* cnt, u32 target) {
#pragma unroll 1
  for (u32 s = 0; s < (SPIN_CAP << 4); ++s) {
    asm volatile("" ::: "memory");
    u32 v = __hip_atomic_load(cnt, __ATOMIC_RELAXED, __HIP_MEMORY_SCOPE_WORKGROUP);
    if (__builtin_amdgcn_readfirstlane(v) >= target) break;
    __builtin_amdgcn_s_sleep(1);
  }
  asm volatile("" ::: "memory");
}

template <int N, class F> DEVI void gather(u32x4 (&v)[N], __amdgpu_buffer_rsrc_t rs, u32 tag, F off GA) {
#pragma unroll 1
  for (u32 s = 0; s < SPIN_CAP; ++s) {
    asm volatile("" ::: "memory");
#pragma unroll
    for (int i = 0; i < N; ++i) v[i] = __builtin_amdgcn_raw_buffer_load_b128(rs, off(i), 0, AUX_SC1);
    u32 lo = v[0].x, hi = v[0].x;
#pragma unroll
    for (int i = 0; i < N; ++i) {
      lo &= (v[i].x & v[i].y) & (v[i].z & v[i].w);
      hi |= (v[i].x | v[i].y) | (v[i].z | v[i].w);
    }
    if (__all((((lo ^ tag) | (hi ^ tag)) & 0xFFu) == 0)) break;
  }
}

struct __attribute__((aligned(16))) Smem {
  float opart[4][8][128];
  float fpart[4][256];
  float qpart[4][128];
  float ml[4][8][2];
  u32 qbf[512];
  u32 hbf[64];
  u16 obf[128];
  float gate[2];
  u32 gen;
  u32 cnt[13];
};
enum { C_Q = 0, C_O = 1, C_O2 = 2, C_P = 3, C_F = 4, C_H = 5, C_T = 6, C_PA = 10, C_OA = 11, C_FA = 12 };

__global__ void __launch_bounds__(640, 3)
fused_decode(const u16* x, const u16* kc, const u16* vc, const u16* wq, const u16* wo, const u16* wg, const u16* wu,
             const u16* wd, const u16* gw, u16* out, u32* ws) {
  __shared__ Smem sm;
  const int lane = threadIdx.x & 63;
  const int wave = __builtin_amdgcn_readfirstlane(threadIdx.x >> 6);
  const int c = blockIdx.x, j = c & 63, cg = c >> 6;
  const int w = wave & 3;
  const bool heavy = j < 56;
  ST(0);
  const __amdgpu_buffer_rsrc_t rs = __builtin_amdgcn_make_buffer_rsrc((void*)ws, 0, WS_BYTES, 0x00020000);
  const int m16 = lane & 15, g4 = lane >> 4;
  const int arow = (g4 * 2 + ((m16 >> 1) & 1)) * 8;
  const int trow = (lane >> 3) * 8, tcol = (lane & 7) * 8;

  if (__builtin_expect(wave < 4, 1)) {
    __builtin_amdgcn_s_setprio(2);
    const int bk = c & 7, sp = c >> 3;
    u32x4 xa = *(const u32x4*)(x + (m16 & 1) * DM + cg * 256 + w * 64 + arow);
    u32x4 rq[8], rk[8], rv[8];
    {
      const u16* p = wq + (size_t)(cg * 256 + w * 64 + trow) * NQ + j * 64 + tcol;
#pragma unroll
      for (int i = 0; i < 8; ++i) rq[i] = ldnt(p + i * NQ);
    }
    PIN();
    ST(11);
    const size_t kvb = ((size_t)bk * SK + (SK - WW) + sp * 128 + w * 32) * DH;
    {
      const u16* p = kc + kvb + (8 * (m16 >> 2) + (m16 & 3)) * DH + g4 * 8;
#pragma unroll
      for (int i = 0; i < 8; ++i) { if (!(i & 1)) VMW(AWIN); rk[i] = ldnt(p + (i >> 2) * 4 * DH + (i & 3) * 32); PIN(); }
    }
    VMW(8);
    ST(1);
    {
      float res[8];
      gemv_tile(rq, xa, lane, res);
      if (lane < 16) {
        *(f32x4*)&sm.qpart[w][lane * 8] = (f32x4){res[0], res[1], res[2], res[3]};
        *(f32x4*)&sm.qpart[w][lane * 8 + 4] = (f32x4){res[4], res[5], res[6], res[7]};
      }
    }
    BAR();
    lds_signal(&sm.cnt[C_P], lane);
    PIN();
    {
      const u16* p = vc + kvb + g4 * 8 * DH + m16 * 8;
#pragma unroll
      for (int i = 0; i < 8; ++i) { VMW(AWIN); rv[i] = ldnt(p + i * DH); PIN(); }
    }
    PIN();
    ST(2);
    VMW(TOK1);
    lds_signal(&sm.cnt[C_T + w], lane);
    VMW(TOK2);
    lds_signal(&sm.cnt[C_T + w], lane);
    ST(7);
    u32x4 fv[8];
#pragma unroll
    for (int cc = 0; cc < 8; ++cc) { fv[cc] = colfrag(rv, cc); asm volatile("" : "+v"(fv[cc])); }
    lds_wait(&sm.cnt[C_Q], 1);
    const u32 gen = __builtin_amdgcn_readfirstlane(sm.gen), tag = gen & 0xFFu;
    ST(3);
    {
      f32x4 s0 = {0.f, 0.f, 0.f, 0.f}, s1 = {0.f, 0.f, 0.f, 0.f};
#pragma unroll
      for (int t = 0; t < 4; ++t) {
        const u32x4 qf = *(const u32x4*)&sm.qbf[(m16 & 7) * 64 + t * 16 + g4 * 4];
        s0 = mfma(rk[t], qf, s0);
        s1 = mfma(rk[4 + t], qf, s1);
      }
      const float sc = 1.4426950408889634f;
      float p[8] = {s0[0], s0[1], s0[2], s0[3], s1[0], s1[1], s1[2], s1[3]};
#pragma unroll
      for (int e = 0; e < 8; e += 2) {
        rb2(p[e], p[e + 1]);
        p[e] *= 0.08838834764831845f; p[e + 1] *= 0.08838834764831845f;
        rb2(p[e], p[e + 1]);
      }
      float mx = fmaxf(fmaxf(fmaxf(p[0], p[1]), fmaxf(p[2], p[3])), fmaxf(fmaxf(p[4], p[5]), fmaxf(p[6], p[7])));
      mx = max32(max16(mx)) * sc;
#pragma unroll
      for (int e = 0; e < 8; ++e) p[e] = __builtin_amdgcn_exp2f(fmaf(p[e], sc, -mx));
      float l = ((p[0] + p[1]) + (p[2] + p[3])) + ((p[4] + p[5]) + (p[6] + p[7]));
      l = sum16(l, l);
      l = sum32(l, l);
      const u32x4 pf = {pkbf(p[0], p[1]), pkbf(p[2], p[3]), pkbf(p[4], p[5]), pkbf(p[6], p[7])};
      f32x4 d[8];
#pragma unroll
      for (int cc = 0; cc < 8; ++cc) d[cc] = mfma(pf, fv[cc], (f32x4){0.f, 0.f, 0.f, 0.f});
      const bool lo = lane < 32;
#pragma unroll
      for (int r = 0; r < 4; ++r)
        *(f32x4*)&sm.opart[w][(g4 & 1) * 4 + r][m16 * 8 + (lo ? 0 : 4)] =
            (f32x4){lo ? d[0][r] : d[4][r], lo ? d[1][r] : d[5][r], lo ? d[2][r] : d[6][r], lo ? d[3][r] : d[7][r]};
      if (lane < 8) { sm.ml[w][lane][0] = mx; sm.ml[w][lane][1] = l; }
    }
    lds_signal(&sm.cnt[C_O], lane);
    if (w < 2) {
      lds_wait(&sm.cnt[C_O], 4);
#pragma unroll
      for (int r = 0; r < 2; ++r) {
        const int h = 4 * w + 2 * r + (lane >> 5), dd = (lane & 31) * 4;
        float mw[4], lw[4];
        f32x4 ow[4];
#pragma unroll
        for (int i = 0; i < 4; ++i) { mw[i] = sm.ml[i][h][0]; lw[i] = sm.ml[i][h][1]; ow[i] = *(f32x4*)&sm.opart[i][h][dd]; }
        const u32 mt = tagf(fmaxf(fmaxf(mw[0], mw[1]), fmaxf(mw[2], mw[3])), tag);
        const float M = untag(mt);
        f32x4 o = {0.f, 0.f, 0.f, 0.f};
        float L = 0.f;
#pragma unroll
        for (int i = 0; i < 4; ++i) { const float f = __builtin_amdgcn_exp2f(mw[i] - M); o += ow[i] * f; L += lw[i] * f; }
        u32x4 ot = {tagf(o[0], tag), tagf(o[1], tag), tagf(o[2], tag), tagf(o[3], tag)};
        __builtin_amdgcn_raw_buffer_store_b128(ot, rs, OP_OFF + (c * 1024 + h * 128 + dd) * 4, 0, AUX_SC1);
        if ((lane & 31) == 0)
          __builtin_amdgcn_raw_buffer_store_b128((u32x4){mt, tagf(L, tag), tagf(L, tag), mt}, rs, ML_OFF + ((bk * 8 + h) * 32 + sp) * 16, 0, AUX_SC1);
      }
      ST(4);
      VMW(0);
      lds_signal(&sm.cnt[C_OA], lane);
      ST(10);
    } else {
      const int pbk = (w - 2) * 4 + (j >> 4), hh = (j >> 1) & 7;
      const int off = OP_OFF + ((pbk + 8 * g4) * 1024 + hh * 128 + (j & 1) * 64 + m16 * 4) * 4;
      const int moff = ML_OFF + ((pbk * 8 + hh) * 32 + g4 + 4 * (m16 & 7)) * 16;
      const float gg = sm.gate[w - 2];
      if (HA2) lds_wait(&sm.cnt[C_OA], 2); else lds_wait(&sm.cnt[C_O], 4);
      if (D2) __builtin_amdgcn_s_sleep(D2);
      u32x4 v[9];
      gather<9>(v, rs, tag, [&](int i) { return i < 8 ? off + i * 32 * 4096 : moff; } GP(8));
      ST(5);
      const float ms = untag(v[8].x);
      float M = fmaxf(ms, ror<4>(ms));
      M = fmaxf(M, ror<2>(M)); M = fmaxf(M, ror<1>(M));
      M = max32(max16(M));
      const float f = __builtin_amdgcn_exp2f(ms - M);
      float L = f * untag(v[8].y);
      L += ror<4>(L); L += ror<2>(L); L += ror<1>(L);
      L = sum16(L, L);
      L = sum32(L, L);
      float o[4] = {0.f, 0.f, 0.f, 0.f};
#define ACC(i) { const float wi = bcast<i>(f); for (int e = 0; e < 4; ++e) o[e] = fmaf(wi, untag(v[i][e]), o[e]); }
      ACC(0) ACC(1) ACC(2) ACC(3) ACC(4) ACC(5) ACC(6) ACC(7)
      const float oo = sum16(sum32(o[0], o[2]), sum32(o[1], o[3])) * (gg / L);
      sm.obf[(w - 2) * 64 + m16 * 4 + g4] = (u16)pkbf(oo, oo);
      lds_signal(&sm.cnt[C_O2], lane);
    }
    if (!heavy) {
      const int b = w & 1, col = (j - 56) * 32 + (w >> 1) * 16;
      const int off = RP_OFF + (((cg * 64 + (lane >> 2)) * 2 + b) * 256 + col + (lane & 3) * 4) * 4;
      lds_wait(&sm.cnt[C_O2], 2);
      if (D3) __builtin_amdgcn_s_sleep(D3);
      u32x4 v[4];
      gather<4>(v, rs, tag, [&](int i) { return off + i * 16 * 2048; } GP(12));
      float o[4];
#pragma unroll
      for (int e = 0; e < 4; ++e) o[e] = (untag(v[0][e]) + untag(v[1][e])) + (untag(v[2][e]) + untag(v[3][e]));
      float t = sum16(sum32(o[0], o[2]), sum32(o[1], o[3]));
      t += ror<8>(t);
      t += ror<4>(t);
      if (m16 < 4) out[b * DM + cg * 256 + col + m16 * 4 + g4] = (u16)pkbf(t, t);
      if (c == 63 && w == 0 && lane == 0) __hip_atomic_store(ws, gen, __ATOMIC_RELAXED, __HIP_MEMORY_SCOPE_AGENT);
      ST(6);
    }
  } else if (wave < 8) {
    u32x4 ro[8], rd[8];
    const size_t to = (size_t)(j * 64 + trow) * DM + cg * 256 + w * 64 + tcol;
    float res[8];
    if (heavy) {
      const u32x4 xa = *(const u32x4*)(x + (m16 & 1) * DM + cg * 256 + w * 64 + arow);
      u32x4 rg[8], ru[8];
      const size_t fo = (size_t)(cg * 256 + w * 64 + trow) * FF + j * 64 + tcol;
      BAR();
      lds_wait(&sm.cnt[C_T + w], 1);
      ST(1);
#pragma unroll
      for (int i = 0; i < 3; ++i) rg[i] = ldnt(wg + fo + i * FF);
      PIN();
      lds_wait(&sm.cnt[C_T + w], 2);
#pragma unroll
      for (int i = 3; i < 8; ++i) { VMW(FWIN); rg[i] = ldnt(wg + fo + i * FF); PIN(); }
#pragma unroll
      for (int i = 0; i < 8; ++i) { VMW(FWIN); ru[i] = ldnt(wu + fo + i * FF); PIN(); }
      ST(2);
      gemv_tile(rg, xa, lane, res);
      if (lane < 16) {
        *(f32x4*)&sm.fpart[w][lane * 8] = (f32x4){res[0], res[1], res[2], res[3]};
        *(f32x4*)&sm.fpart[w][lane * 8 + 4] = (f32x4){res[4], res[5], res[6], res[7]};
      }
      PIN();
#pragma unroll
      for (int i = 0; i < 8; ++i) { VMW(FWIN); ro[i] = ldnt(wo + to + i * DM); PIN(); }
      ST(3);
      gemv_tile(ru, xa, lane, res);
      if (lane < 16) {
        *(f32x4*)&sm.fpart[w][128 + lane * 8] = (f32x4){res[0], res[1], res[2], res[3]};
        *(f32x4*)&sm.fpart[w][128 + lane * 8 + 4] = (f32x4){res[4], res[5], res[6], res[7]};
      }
      lds_signal(&sm.cnt[C_F], lane);
      PIN();
#pragma unroll
      for (int i = 0; i < 8; ++i) { VMW(FWIN); rd[i] = ldnt(wd + to + i * DM); PIN(); }
#pragma unroll
      for (int i = 0; i < 8; ++i) asm volatile("" : "+v"(ro[i]));
    } else {
      BAR();
      lds_wait(&sm.cnt[C_T + w], 1);
#pragma unroll
      for (int i = 0; i < 8; ++i) ro[i] = ldnt(wo + to + i * DM);
#pragma unroll
      for (int i = 0; i < 8; ++i) asm volatile("" : "+v"(ro[i]));
    }
    ST(4);
    u32x4 fo[8];
    f32x4 d[8];
#pragma unroll
    for (int cc = 0; cc < 8; ++cc) { fo[cc] = colfrag(ro, cc); asm volatile("" : "+v"(fo[cc])); d[cc] = (f32x4){0.f, 0.f, 0.f, 0.f}; }
    if (heavy) {
      u32x4 fd[8];
#pragma unroll
      for (int cc = 0; cc < 8; ++cc) { fd[cc] = colfrag(rd, cc); asm volatile("" : "+v"(fd[cc])); }
      ST(5);
      lds_wait(&sm.cnt[C_H], 1);
      const u32x4 ha = *(const u32x4*)&sm.hbf[(m16 & 1) * 32 + (arow >> 1)];
#pragma unroll
      for (int cc = 0; cc < 8; ++cc) d[cc] = mfma(ha, fd[cc], d[cc]);
    }
    lds_wait(&sm.cnt[C_O2], 2);
    ST(6);
    const u32 tag = __builtin_amdgcn_readfirstlane(sm.gen) & 0xFFu;
    {
      const u32x4 oa = *(const u32x4*)&sm.obf[(m16 & 1) * 64 + arow];
      const bool lo = (lane & 8) == 0;
#pragma unroll
      for (int cc = 0; cc < 8; ++cc) {
        d[cc] = mfma(oa, fo[cc], d[cc]);
        res[cc] = (lo ? d[cc][0] : d[cc][3]) + ror<8>(lo ? d[cc][1] : d[cc][2]);
      }
    }
    {
      u32x4 o;
#pragma unroll
      for (int e = 0; e < 4; ++e) o[e] = tagf(asf(__builtin_amdgcn_permlane16_swap(asu(res[e]), asu(res[4 + e]), false, false)[0]), tag);
      if (lane < 32) __builtin_amdgcn_raw_buffer_store_b128(o, rs, RP_OFF + ((c * 2 + (m16 >> 3)) * 256 + w * 64 + tcol + g4 * 4) * 4, 0, AUX_SC1);
    }
    ST(7);
  } else if (wave == 9) {
    __builtin_amdgcn_s_setprio(3);
    const u32 ev = __hip_atomic_load(ws, __ATOMIC_RELAXED, __HIP_MEMORY_SCOPE_AGENT);
    BAR();
    const u32 tag = (__builtin_amdgcn_readfirstlane(ev) + 1u) & 0xFFu;
    lds_wait(&sm.cnt[C_P], 4);
    if (lane < 32) {
      const int e = lane * 4;
      f32x4 s = (*(f32x4*)&sm.qpart[0][e] + *(f32x4*)&sm.qpart[1][e]) + (*(f32x4*)&sm.qpart[2][e] + *(f32x4*)&sm.qpart[3][e]);
      u32x4 o = {tagf(s[0], tag), tagf(s[1], tag), tagf(s[2], tag), tagf(s[3], tag)};
      __builtin_amdgcn_raw_buffer_store_b128(o, rs, QP_OFF + (((e >> 6) * 4 + cg) * NQ + j * 64 + (e & 63)) * 4, 0, AUX_SC1);
    }
    VMW(0);
    lds_signal(&sm.cnt[C_PA], lane);
    ST(7);
    if (heavy) {
      lds_wait(&sm.cnt[C_F], 4);
      const f32x4 s = (*(f32x4*)&sm.fpart[0][lane * 4] + *(f32x4*)&sm.fpart[1][lane * 4]) + (*(f32x4*)&sm.fpart[2][lane * 4] + *(f32x4*)&sm.fpart[3][lane * 4]);
      __builtin_amdgcn_raw_buffer_store_b128((u32x4){tagf(s[0], tag), tagf(s[1], tag), tagf(s[2], tag), tagf(s[3], tag)}, rs, FP_OFF + (j * 1024 + cg * 256 + lane * 4) * 4, 0, AUX_SC1);
      ST(6);
      VMW(0);
      lds_signal(&sm.cnt[C_FA], lane);
    }
  } else {
    __builtin_amdgcn_s_setprio(3);
    const u32 ev = __hip_atomic_load(ws, __ATOMIC_RELAXED, __HIP_MEMORY_SCOPE_AGENT);
    if (lane < 13) sm.cnt[lane] = 0u;
    {
      float acc[2] = {0.f, 0.f};
#pragma unroll
      for (int i = 0; i < 2; ++i) {
        const u32x4 gv = *(const u32x4*)(gw + i * 512 + lane * 8);
#pragma unroll
        for (int b = 0; b < 2; ++b) {
          const u32x4 xv = *(const u32x4*)(x + b * DM + i * 512 + lane * 8);
#pragma unroll
          for (int e = 0; e < 4; ++e) {
            float p0 = asf(gv[e] << 16) * asf(xv[e] << 16), p1 = asf(gv[e] & 0xFFFF0000u) * asf(xv[e] & 0xFFFF0000u);
            rb2(p0, p1);
            acc[b] += p0 + p1;
          }
        }
      }
      float t = sum32(acc[0], acc[1]);
      t = sum16(t, t);
      t += ror<8>(t); t += ror<4>(t); t += ror<2>(t); t += ror<1>(t);
      if ((lane & 31) == 0) sm.gate[lane >> 5] = rb1(1.f / (1.f + __expf(-rb1(t))));
    }
    const u32 gen = __builtin_amdgcn_readfirstlane(ev) + 1u, tag = gen & 0xFFu;
    if (lane == 0) sm.gen = gen;
    BAR();
    if (HA1) lds_wait(&sm.cnt[C_PA], 1); else lds_wait(&sm.cnt[C_P], 4);
    ST(4);
    if (D1) __builtin_amdgcn_s_sleep(D1);
    {
      const int bk = c & 7;
      const int off = QP_OFF + ((bk >> 2) * 4 * NQ + (bk & 3) * 1024 + lane * 4) * 4;
      u32x4 v[16];
      gather<16>(v, rs, tag, [&](int i) { return off + (i & 3) * NQ * 4 + (i >> 2) * 1024; } GP(8));
      ST(5);
#pragma unroll
      for (int i = 0; i < 4; ++i) {
        float q[4];
#pragma unroll
        for (int e = 0; e < 4; ++e) q[e] = (untag(v[4 * i][e]) + untag(v[4 * i + 1][e])) + (untag(v[4 * i + 2][e]) + untag(v[4 * i + 3][e]));
        *(u32x2*)&sm.qbf[i * 128 + lane * 2] = (u32x2){pkbf(q[0], q[1]), pkbf(q[2], q[3])};
      }
    }
    lds_signal(&sm.cnt[C_Q], lane);
    if (heavy) {
      lds_wait(&sm.cnt[C_FA], 1);
      ST(1);
      const int off = FP_OFF + (j * 1024 + lane * 4) * 4;
      if (D4) __builtin_amdgcn_s_sleep(D4);
      u32x4 v[4];
      gather<4>(v, rs, tag, [&](int i) { return off + i * 1024; } GP(10));
      ST(2);
      float h[4];
      const float g1 = 0.5f * rb1(1.f - sm.gate[(lane >> 4) & 1]);
#pragma unroll
      for (int e = 0; e < 4; ++e) {
        const float t = (untag(v[0][e]) + untag(v[1][e])) + (untag(v[2][e]) + untag(v[3][e]));
        auto r = __builtin_amdgcn_permlane32_swap(asu(t), asu(t), false, false);
        const float gt = asf(r[0]), up = asf(r[1]);
        const float y = 0.7978845608028654f * (gt + 0.044715f * gt * gt * gt);
        const float th = 1.f - 2.f / (__expf(2.f * y) + 1.f);
        h[e] = g1 * gt * (1.f + th) * up;
      }
      if (lane < 32) *(u32x2*)&sm.hbf[lane * 2] = (u32x2){pkbf(h[0], h[1]), pkbf(h[2], h[3])};
      lds_signal(&sm.cnt[C_H], lane);
      ST(3);
    }
  }
}

extern "C" void kernel_launch(void* const* d_in, const int* in_sizes, int n_in, void* d_out, int out_size, void* d_ws,
                              size_t ws_size, hipStream_t stream) {
  fused_decode<<<256, 640, 0, stream>>>((const u16*)d_in[0], (const u16*)d_in[1], (const u16*)d_in[2], (const u16*)d_in[3],
                                        (const u16*)d_in[4], (const u16*)d_in[6], (const u16*)d_in[7], (const u16*)d_in[8],
                                        (const u16*)d_in[9], (u16*)d_out, (u32*)d_ws);
}
```
